```python
import torch

def _softmax(x, axis=-1):
    return torch.softmax(x, dim=axis)
def _gelu(x):
    return torch.nn.functional.gelu(x, approximate="tanh")
def _silu(x):
    return torch.nn.functional.silu(x)
def _sigmoid(x):
    return torch.sigmoid(x)
def _layernorm(x, axis=-1, eps=1e-5):
    m = x.mean(dim=axis, keepdim=True)
    v = x.var(dim=axis, keepdim=True, unbiased=False)
    return (x - m) / torch.sqrt(v + eps)
def _scatter_add(tgt, idx, src, axis=0):
    return tgt.clone().index_add_(axis, idx, src)
def _segment_sum(src, seg, n):
    out = torch.zeros((n,) + src.shape[1:], dtype=src.dtype)
    return out.index_add_(0, seg, src)
def _pad(x, pw):
    flat = tuple(v for pair in reversed(pw) for v in pair)
    return torch.nn.functional.pad(x, flat)

kernel_name = 'standalone__gqa_block_cached__near_ridge'

def setup_inputs():

    _w = (lambda a: (lambda t: t.to(torch.bfloat16) if t.is_floating_point() else t)(torch.as_tensor(a)))
    B, Sq, D = 8, 112, 576
    Hq, Hkv, Sk, Dh = 32, 16, 512, 128
    assert Hq % Hkv == 0, (Hq, Hkv)
    return {
        "x": _w((lambda *s: torch.randn(*s, dtype=torch.bfloat16) * 0.8)(B, Sq, D)),
        "k_cache": _w((lambda *s: torch.randn(*s, dtype=torch.bfloat16) * 0.8)(B, Hkv, Sk, Dh)),
        "v_cache": _w((lambda *s: torch.randn(*s, dtype=torch.bfloat16) * 0.05)(B, Hkv, Sk, Dh)),
        "wq": _w((lambda *s: torch.randn(*s, dtype=torch.bfloat16) * 0.8)(D, Hq * Dh) * (D ** -0.5)),
        "wo": _w((lambda *s: torch.randn(*s, dtype=torch.bfloat16) * 0.05)(Hq * Dh, D) * ((Hq * Dh) ** -0.5)),
    }

def reference(x, k_cache, v_cache, wq, wo):
    B, Sq, D = x.shape
    Hq, Dh = 32, 128
    Hkv = k_cache.shape[1]
    rep = Hq // Hkv
    q = (lambda x, *d: x.permute(*d))((x @ wq).reshape(B, Sq, Hq, Dh), 0, 2, 1, 3)
    k = (lambda x, n, axis: torch.repeat_interleave(x, n, dim=axis))(k_cache, rep, axis=1)
    v = (lambda x, n, axis: torch.repeat_interleave(x, n, dim=axis))(v_cache, rep, axis=1)
    scores = torch.einsum("bhqd,bhkd->bhqk", q, k) * (Dh ** -0.5)
    p = _softmax(scores, axis=-1)
    o = torch.einsum("bhqk,bhkd->bhqd", p, v)
    return (lambda x, *d: x.permute(*d))(o, 0, 2, 1, 3).reshape(B, Sq, Hq * Dh) @ wo
```

```cpp
#include <hip/hip_runtime.h>
#include <hip/hip_bf16.h>
typedef __hip_bfloat16 bf16;
constexpr int B=8,SQ=112,D=576,HQ=32,HKV=16,SK=512,DH=128;

__global__ void qproj(const bf16* x, const bf16* wq, bf16* q){
  int n = blockIdx.x*256+threadIdx.x; int m = blockIdx.y;
  float acc=0; for(int k=0;k<D;++k) acc += __bfloat162float(x[m*D+k])*__bfloat162float(wq[(size_t)k*4096+n]);
  q[(size_t)m*4096+n]=__float2bfloat16(acc);
}
__global__ void attn(const bf16* q,const bf16* k,const bf16* v,bf16* o){
  int s=blockIdx.x,h=blockIdx.y,b=blockIdx.z,t=threadIdx.x;
  __shared__ float qs[128]; __shared__ float p[512]; __shared__ float red[128];
  qs[t]=__bfloat162float(q[(size_t)(b*SQ+s)*4096+h*128+t]); __syncthreads();
  const bf16* kk=k+(size_t)(b*HKV+h/2)*SK*DH; const bf16* vv=v+(size_t)(b*HKV+h/2)*SK*DH;
  float mx=-1e30f;
  for(int j=t;j<SK;j+=128){ float a=0; for(int d=0;d<128;++d) a+=qs[d]*__bfloat162float(kk[j*DH+d]); a*=0.08838834764f; p[j]=a; mx=fmaxf(mx,a);}
  red[t]=mx; __syncthreads(); for(int o_=64;o_>0;o_>>=1){ if(t<o_) red[t]=fmaxf(red[t],red[t+o_]); __syncthreads();}
  mx=red[0]; __syncthreads();
  float sm=0; for(int j=t;j<SK;j+=128){ float e=__expf(p[j]-mx); p[j]=e; sm+=e;}
  red[t]=sm; __syncthreads(); for(int o_=64;o_>0;o_>>=1){ if(t<o_) red[t]+=red[t+o_]; __syncthreads();}
  sm=red[0];
  float a=0; for(int j=0;j<SK;++j) a+=p[j]*__bfloat162float(vv[j*DH+t]);
  o[(size_t)(b*SQ+s)*4096+h*128+t]=__float2bfloat16(a/sm);
}
__global__ void oproj(const bf16* o,const bf16* wo,bf16* out){
  int n=blockIdx.x*64+threadIdx.x; int m=blockIdx.y;
  float acc=0; for(int k=0;k<4096;++k) acc+=__bfloat162float(o[(size_t)m*4096+k])*__bfloat162float(wo[(size_t)k*D+n]);
  out[m*D+n]=__float2bfloat16(acc);
}
extern "C" void kernel_launch(void* const* d_in, const int* in_sizes, int n_in, void* d_out, int out_size, void* d_ws, size_t ws_size, hipStream_t stream) {
  const bf16* x=(const bf16*)d_in[0]; const bf16* k=(const bf16*)d_in[1]; const bf16* v=(const bf16*)d_in[2]; const bf16* wq=(const bf16*)d_in[3]; const bf16* wo=(const bf16*)d_in[4];
  bf16* q=(bf16*)d_ws; bf16* o=q+(size_t)896*4096;
  qproj<<<dim3(16,896),256,0,stream>>>(x,wq,q);
  attn<<<dim3(SQ,HQ,B),128,0,stream>>>(q,k,v,o);
  oproj<<<dim3(9,896),64,0,stream>>>(o,wo,(bf16*)d_out);
}
```

```cpp
#include <hip/hip_runtime.h>
#include <stdint.h>
typedef unsigned short u16;
typedef short bf16x8 __attribute__((ext_vector_type(8)));
typedef short s16x4 __attribute__((ext_vector_type(4)));
typedef float f32x16 __attribute__((ext_vector_type(16)));
typedef float f32x4 __attribute__((ext_vector_type(4)));
typedef float f32x2 __attribute__((ext_vector_type(2)));
typedef __bf16 b16x2 __attribute__((ext_vector_type(2)));
typedef unsigned u32x4 __attribute__((ext_vector_type(4)));
typedef unsigned u32x2 __attribute__((ext_vector_type(2)));
#define LAS __attribute__((address_space(3)))
#define DI __device__ __forceinline__

constexpr int PG = 16384;
constexpr size_t WS_O_OFF = 0;
constexpr size_t WS_P_OFF = 8u << 20;
#define O_PERM(d) (((d) & ~31) | ((((d) >> 2) & 3) << 3) | ((((d) >> 4) & 1) << 2) | ((d) & 3))

DI void glds16(unsigned voff, const void* sbase, unsigned lds_dst) {
  unsigned keep;
  asm volatile("s_mov_b32 %0, m0\n\ts_mov_b32 m0, %3\n\ts_nop 0\n\tglobal_load_lds_dwordx4 %1, %2\n\ts_mov_b32 m0, %0"
               : "=&s"(keep) : "v"(voff), "s"(sbase), "s"(lds_dst) : "memory");
}
#define WAITV(n) asm volatile("s_waitcnt vmcnt(" #n ")" ::: "memory")
#define BAR() asm volatile("s_waitcnt lgkmcnt(0)\n\ts_barrier" ::: "memory")
DI unsigned pk2(float a, float b) { f32x2 f = {a, b}; return __builtin_bit_cast(unsigned, __builtin_convertvector(f, b16x2)); }
DI bf16x8 ldsr128(LAS char* base, unsigned off) { return *(LAS bf16x8*)(base + off); }
DI bf16x8 ldstr(LAS char* base, unsigned off0, unsigned off1) {
  s16x4 a = __builtin_amdgcn_ds_read_tr16_b64_v4i16((LAS s16x4*)(base + off0));
  s16x4 b = __builtin_amdgcn_ds_read_tr16_b64_v4i16((LAS s16x4*)(base + off1));
  return __builtin_shufflevector(a, b, 0, 1, 2, 3, 4, 5, 6, 7);
}
#define MFMA32(a, b, c) __builtin_amdgcn_mfma_f32_32x32x16_bf16((a), (b), (c), 0, 0, 0)
#define MFMA16(a, b, c) __builtin_amdgcn_mfma_f32_16x16x32_bf16((a), (b), (c), 0, 0, 0)

__global__ __launch_bounds__(512, 2) void k_attn(const u16* __restrict__ x, const u16* __restrict__ kc, const u16* __restrict__ vc,
                                                 const u16* __restrict__ wq, u16* __restrict__ Ows) {
  extern __shared__ __attribute__((aligned(16))) char smem_[];
  LAS char* smem = (LAS char*)smem_;
  const unsigned lds0 = (unsigned)(size_t)smem;
  const int tid = threadIdx.x, lane = tid & 63;
  const int w = __builtin_amdgcn_readfirstlane(tid >> 6);
  const int bid = blockIdx.x, xcd = bid & 7, jj = bid >> 3;
  const int h = xcd * 4 + (jj & 3), b = jj >> 2;
  const int hh = lane >> 5, l31 = lane & 31, dsub = (lane >> 4) & 1, qq = (lane & 15) >> 2, p3 = lane & 3;
  const u16* xb = x + (size_t)b * 112 * 576;
  const u16* wqh = wq + h * 128;
  const u16* kb = kc + (size_t)(b * 16 + (h >> 1)) * 512 * 128;
  const u16* vb = vc + (size_t)(b * 16 + (h >> 1)) * 512 * 128;
  const int wl = w & 3;
  unsigned xoff[4];
#pragma unroll
  for (int pp = 0; pp < 4; ++pp) {
    int p = 4 * wl + pp; int row = 8 * p + (lane >> 3); int rc = row < 112 ? row : 111;
    int ch = (lane & 7) ^ ((4 * (p & 1) + (lane >> 4)) & 7);
    xoff[pp] = (unsigned)(rc * 1152 + ch * 16);
  }
  const unsigned woff = (unsigned)((16 * wl + (lane >> 4)) * 8192 + (((lane & 15) ^ ((lane >> 4) << 2)) * 16));
  unsigned koff[4];
#pragma unroll
  for (int pp = 0; pp < 4; ++pp) {
    int row = 16 * wl + 4 * pp + (lane >> 4);
    koff[pp] = (unsigned)(row * 256 + (((lane & 15) ^ (row & 15)) * 16));
  }
  const unsigned voff = (unsigned)((16 * wl + (lane >> 4)) * 256 + (((lane & 15) ^ ((lane >> 4) << 2)) * 16));

  auto issueW = [&](int c) {
    const unsigned sb = lds0 + (unsigned)(c % 3) * 32768u;
#pragma unroll
    for (int pp = 0; pp < 4; ++pp) glds16(xoff[pp], (const char*)xb + c * 128, sb + (4 * wl + pp) * 1024);
#pragma unroll
    for (int pp = 0; pp < 4; ++pp) glds16(woff, (const char*)wqh + (size_t)(64 * c + 4 * pp) * 8192, sb + 16384 + (4 * wl + pp) * 1024);
  };
  auto issueK = [&](int pg, int page) {
#pragma unroll
    for (int pp = 0; pp < 4; ++pp) glds16(koff[pp], (const char*)kb + pg * 16384, lds0 + page * PG + (4 * wl + pp) * 1024);
  };
  auto issueV = [&](int pg, int page) {
#pragma unroll
    for (int pp = 0; pp < 4; ++pp) glds16(voff, (const char*)vb + pg * 16384 + pp * 1024, lds0 + page * PG + (4 * wl + pp) * 1024);
  };

  if (w < 4) { issueW(0); issueW(1); issueW(2); }
  else { issueK(0, 6); issueK(4, 7); issueK(1, 8); issueK(5, 9); }

  const int dtA = w & 3, qh = w >> 2;
  const unsigned aW = (unsigned)(16384 + (8 * hh + qq) * 256 + 64 * (dtA ^ qq) + 32 * dsub + 8 * p3);
  const unsigned aX = (unsigned)((64 * qh + l31) * 128 + ((hh ^ ((l31 >> 1) & 7)) << 4));
  f32x16 accq[2] = {};
#pragma unroll
  for (int c = 0; c < 9; ++c) {
    if (w < 4) { if (c < 8) { if (c == 0) WAITV(16); else WAITV(8); } else WAITV(0); }
    BAR();
    if (w < 4) { if (c >= 1 && c + 2 <= 8) issueW(c + 2); }
    else { if (c == 7) { issueK(2, 0); issueK(6, 1); } if (c == 8) { issueK(3, 2); issueK(7, 3); } }
    const unsigned sb = (unsigned)(c % 3) * 32768u;
#pragma unroll
    for (int s = 0; s < 4; ++s) {
      bf16x8 A = ldstr(smem, sb + aW + s * 4096, sb + aW + s * 4096 + 1024);
      bf16x8 B0 = ldsr128(smem, sb + (aX ^ (s << 5)));
      bf16x8 B1 = ldsr128(smem, sb + (aX ^ (s << 5)) + 4096);
      accq[0] = MFMA32(A, B0, accq[0]);
      accq[1] = MFMA32(A, B1, accq[1]);
    }
  }
  BAR();
#pragma unroll
  for (int u = 0; u < 2; ++u) {
    const int row = 64 * qh + 32 * u + l31;
#pragma unroll
    for (int g = 0; g < 4; ++g) {
      u32x2 v = {pk2(accq[u][4 * g], accq[u][4 * g + 1]), pk2(accq[u][4 * g + 2], accq[u][4 * g + 3])};
      *(LAS u32x2*)(smem + 4 * PG + row * 256 + (((4 * dtA + g) ^ (row & 15)) << 4) + 8 * hh) = v;
    }
  }
  if (w >= 4) WAITV(24);
  BAR();
  const int kh = w >> 2, qg = w & 3;
  const unsigned aR = (unsigned)(l31 * 256 + ((hh ^ (l31 & 15)) << 4));
  bf16x8 qf[8];
#pragma unroll
  for (int s = 0; s < 8; ++s) qf[s] = ldsr128(smem, 4 * PG + qg * 8192 + (aR ^ (s << 5)));
  f32x16 S[8];
#pragma unroll
  for (int tp = 0; tp < 4; ++tp) {
    if (tp > 0) {
      if (w >= 4) { if (tp == 1) WAITV(16); else if (tp == 2) WAITV(16); else WAITV(16); }
      BAR();
      if (w >= 4) {
        if (tp == 1) { issueV(0, 4); issueV(4, 5); }
        if (tp == 2) { issueV(1, 6); issueV(5, 7); }
        if (tp == 3) { issueV(2, 8); issueV(6, 9); }
      }
    }
    const int pgk = (tp == 0 ? 6 : tp == 1 ? 8 : tp == 2 ? 0 : 2);
#pragma unroll
    for (int tt = 0; tt < 2; ++tt) {
      f32x16 acc = {};
#pragma unroll
      for (int s = 0; s < 8; ++s) {
        bf16x8 A = ldsr128(smem, (unsigned)(pgk * PG) + kh * PG + tt * 8192 + (aR ^ (s << 5)));
        acc = MFMA32(A, qf[s], acc);
      }
      S[2 * tp + tt] = acc;
    }
  }
  BAR();
  if (w >= 4) { issueV(3, 0); issueV(7, 1); }
  float mx = S[0][0];
#pragma unroll
  for (int t = 0; t < 8; ++t)
#pragma unroll
    for (int r = 0; r < 16; ++r) mx = fmaxf(mx, S[t][r]);
  mx = fmaxf(mx, __shfl_xor(mx, 32));
  const float cs = 0.08838834764831845f * 1.4426950408889634f;
  const float nmc = -mx * cs;
  float lsum = 0.f;
  f32x16 O[4] = {};
  unsigned aV[4];
#pragma unroll
  for (int dt = 0; dt < 4; ++dt) aV[dt] = (unsigned)((4 * hh + qq) * 256 + 64 * (dt ^ qq) + 32 * dsub + 8 * p3);
#pragma unroll
  for (int vp = 0; vp < 4; ++vp) {
    if (w >= 4) { if (vp == 0) WAITV(24); else if (vp == 1) WAITV(16); else if (vp == 2) WAITV(8); else WAITV(0); }
    BAR();
    const int pgv = (vp == 0 ? 4 : vp == 1 ? 6 : vp == 2 ? 8 : 0);
#pragma unroll
    for (int tt = 0; tt < 2; ++tt) {
      const int t = 2 * vp + tt;
#pragma unroll
      for (int s = 0; s < 2; ++s) {
        float p[8];
#pragma unroll
        for (int j = 0; j < 8; ++j) { p[j] = __builtin_amdgcn_exp2f(fmaf(S[t][8 * s + j], cs, nmc)); lsum += p[j]; }
        u32x4 pw = {pk2(p[0], p[1]), pk2(p[2], p[3]), pk2(p[4], p[5]), pk2(p[6], p[7])};
        bf16x8 Bp = __builtin_bit_cast(bf16x8, pw);
#pragma unroll
        for (int dt = 0; dt < 4; ++dt) {
          const unsigned a0 = (unsigned)(pgv * PG) + kh * PG + tt * 8192 + s * 4096 + aV[dt];
          bf16x8 A = ldstr(smem, a0, a0 + 2048);
          O[dt] = MFMA32(A, Bp, O[dt]);
        }
      }
    }
  }
  lsum += __shfl_xor(lsum, 32);
  f32x16 Ok0, Ok1;
  {
    f32x16 Os0, Os1;
    if (kh == 0) { Ok0 = O[0]; Ok1 = O[1]; Os0 = O[2]; Os1 = O[3]; } else { Ok0 = O[2]; Ok1 = O[3]; Os0 = O[0]; Os1 = O[1]; }
#pragma unroll
    for (int g = 0; g < 4; ++g) {
      f32x4 v = {Os0[4 * g], Os0[4 * g + 1], Os0[4 * g + 2], Os0[4 * g + 3]};
      *(LAS f32x4*)(smem + 2 * PG + w * 8192 + g * 1024 + lane * 16) = v;
      f32x4 v2 = {Os1[4 * g], Os1[4 * g + 1], Os1[4 * g + 2], Os1[4 * g + 3]};
      *(LAS f32x4*)(smem + 2 * PG + w * 8192 + (4 + g) * 1024 + lane * 16) = v2;
    }
    f32x2 st = {mx, lsum};
    *(LAS f32x2*)(smem + 6 * PG + w * 512 + lane * 8) = st;
  }
  BAR();
  {
    const int pw_ = w ^ 4;
    f32x2 st = *(LAS f32x2*)(smem + 6 * PG + pw_ * 512 + lane * 8);
    const float m2 = fmaxf(mx, st[0]);
    const float a1 = __builtin_amdgcn_exp2f((mx - m2) * cs), a2 = __builtin_amdgcn_exp2f((st[0] - m2) * cs);
    const float inv = 1.f / (a1 * lsum + a2 * st[1]);
    const float f1 = a1 * inv, f2 = a2 * inv;
    const int row = 32 * qg + l31;
#pragma unroll
    for (int u = 0; u < 2; ++u) {
      const int dt = 2 * kh + u;
      float r[16];
#pragma unroll
      for (int g = 0; g < 4; ++g) {
        f32x4 v = *(LAS f32x4*)(smem + 2 * PG + pw_ * 8192 + (u * 4 + g) * 1024 + lane * 16);
#pragma unroll
        for (int e = 0; e < 4; ++e) r[4 * g + e] = (u == 0 ? Ok0[4 * g + e] : Ok1[4 * g + e]) * f1 + v[e] * f2;
      }
#pragma unroll
      for (int g1 = 0; g1 < 2; ++g1) {
        u32x4 v = {pk2(r[4 * g1], r[4 * g1 + 1]), pk2(r[4 * g1 + 2], r[4 * g1 + 3]), pk2(r[4 * (g1 + 2)], r[4 * (g1 + 2) + 1]), pk2(r[4 * (g1 + 2) + 2], r[4 * (g1 + 2) + 3])};
        *(LAS u32x4*)(smem + row * 256 + (((4 * dt + 2 * g1 + hh) ^ (row & 15)) << 4)) = v;
      }
    }
  }
  BAR();
  {
    char* og = (char*)Ows + (size_t)(b * 32 + h) * 112 * 256;
#pragma unroll
    for (int i = 0; i < 4; ++i) {
      const int p = w + 8 * i;
      if (p < 28) {
        const int row = 4 * p + (lane >> 4), pos = lane & 15;
        u32x4 v = *(LAS u32x4*)(smem + row * 256 + pos * 16);
        *(u32x4*)(og + row * 256 + ((pos ^ (row & 15)) << 4)) = v;
      }
    }
  }
}

__global__ __launch_bounds__(512, 2) void k_oproj(const u16* __restrict__ Ows, const u16* __restrict__ wo, float* __restrict__ part) {
  extern __shared__ __attribute__((aligned(16))) char smem_[];
  LAS char* smem = (LAS char*)smem_;
  const unsigned lds0 = (unsigned)(size_t)smem;
  const int tid = threadIdx.x, lane = tid & 63;
  const int w = __builtin_amdgcn_readfirstlane(tid >> 6);
  const int bid = blockIdx.x, ks = bid & 7, jj = bid >> 3, nq = jj & 3, b = jj >> 2;
  const char* Ob = (const char*)Ows + (size_t)(b * 32 + ks * 4) * 112 * 256;
  const char* wob = (const char*)wo + ((size_t)ks * 512 * 576 + nq * 144) * 2;
  unsigned off[8];
#pragma unroll
  for (int i = 0; i < 8; ++i) {
    const int P = w * 8 + i;
    if (P < 28) { int row = 4 * P + (lane >> 4); off[i] = (unsigned)(row * 256 + (((lane & 15) ^ (row & 15)) << 4)); }
    else { int o = (P - 28) * 1024 + lane * 16; int row = o / 288, cb = o % 288; off[i] = (unsigned)(row * 1152 + cb); }
  }
  auto issue = [&](int st) {
    const unsigned sb = lds0 + (unsigned)(st & 1) * 65536u;
#pragma unroll
    for (int i = 0; i < 8; ++i) {
      const int P = w * 8 + i;
      if (P < 28) glds16(off[i], Ob + (size_t)st * 112 * 256, sb + P * 1024);
      else glds16(off[i], wob + (size_t)st * 128 * 1152, sb + P * 1024);
    }
  };
  issue(0); issue(1);
  const int ng = w & 3, qg = w >> 2;
  const int n0 = ng == 0 ? 0 : 1 + 2 * ng, nn = ng == 0 ? 3 : 2;
  const int q0 = qg * 4, nqq = qg == 0 ? 4 : 3;
  const int l15 = lane & 15, g = lane >> 4, qq = l15 >> 2, p3 = lane & 3;
  f32x4 acc[3][4] = {};
  const unsigned aO = (unsigned)((16 * q0 + l15) * 256);
  const unsigned aW = (unsigned)(28672 + (4 * g + qq) * 288 + n0 * 32 + 8 * p3);
#pragma unroll
  for (int st = 0; st < 4; ++st) {
    if (st == 3) WAITV(0); else WAITV(8);
    BAR();
    const unsigned sb = (unsigned)(st & 1) * 65536u;
#pragma unroll
    for (int kb = 0; kb < 4; ++kb) {
      bf16x8 Bf[4], Af[3];
#pragma unroll
      for (int u = 0; u < 4; ++u) if (u < nqq) Bf[u] = ldsr128(smem, sb + aO + u * 4096 + (((4 * kb + g) ^ l15) << 4));
#pragma unroll
      for (int i = 0; i < 3; ++i) if (i < nn) Af[i] = ldstr(smem, sb + aW + kb * 32 * 288 + i * 32, sb + aW + kb * 32 * 288 + 16 * 288 + i * 32);
#pragma unroll
      for (int i = 0; i < 3; ++i)
#pragma unroll
        for (int u = 0; u < 4; ++u) if (i < nn && u < nqq) acc[i][u] = MFMA16(Af[i], Bf[u], acc[i][u]);
    }
    if (st + 2 < 4) { BAR(); issue(st + 2); }
  }
  float* pb = part + ((size_t)(ks * 8 + b) * 4 + nq) * 63 * 256;
#pragma unroll
  for (int i = 0; i < 3; ++i)
#pragma unroll
    for (int u = 0; u < 4; ++u) if (i < nn && u < nqq) *(f32x4*)(pb + ((n0 + i) * 7 + q0 + u) * 256 + lane * 4) = acc[i][u];
}

__global__ __launch_bounds__(256) void k_reduce(const float* __restrict__ part, u16* __restrict__ out) {
  const int idx = blockIdx.x * 256 + threadIdx.x;
  const int lane = idx & 63, tile = (idx >> 6) % 63, bn = (idx >> 6) / 63;
  f32x4 s = {0, 0, 0, 0};
#pragma unroll
  for (int ks = 0; ks < 8; ++ks) s += *(const f32x4*)(part + ((size_t)(ks * 32 + bn) * 63 + tile) * 256 + lane * 4);
  const int b = bn >> 2, nq = bn & 3, nt = tile / 7, qt = tile % 7;
  const int q = qt * 16 + (lane & 15), n = nq * 144 + nt * 16 + (lane >> 4) * 4;
  u32x2 v = {pk2(s[0], s[1]), pk2(s[2], s[3])};
  *(u32x2*)(out + (size_t)(b * 112 + q) * 576 + n) = v;
}

extern "C" void kernel_launch(void* const* d_in, const int* in_sizes, int n_in, void* d_out, int out_size, void* d_ws, size_t ws_size, hipStream_t stream) {
  const u16* x = (const u16*)d_in[0]; const u16* k = (const u16*)d_in[1]; const u16* v = (const u16*)d_in[2];
  const u16* wq = (const u16*)d_in[3]; const u16* wo = (const u16*)d_in[4];
  u16* O = (u16*)((char*)d_ws + WS_O_OFF); float* part = (float*)((char*)d_ws + WS_P_OFF);
  static bool init = false;
  if (!init) {
    hipFuncSetAttribute((const void*)k_attn, hipFuncAttributeMaxDynamicSharedMemorySize, 163840);
    hipFuncSetAttribute((const void*)k_oproj, hipFuncAttributeMaxDynamicSharedMemorySize, 131072);
    init = true;
  }
  k_attn<<<256, 512, 163840, stream>>>(x, k, v, wq, O);
  k_oproj<<<256, 512, 131072, stream>>>(O, wo, part);
  k_reduce<<<504, 256, 0, stream>>>(part, (u16*)d_out);
}
```

```cpp
#define RSLEEP 48
#define WO2 3
#include <hip/hip_runtime.h>
#include <stdint.h>
typedef unsigned short u16;
typedef short bf16x8 __attribute__((ext_vector_type(8)));
typedef short s16x4 __attribute__((ext_vector_type(4)));
typedef float f32x16 __attribute__((ext_vector_type(16)));
typedef float f32x4 __attribute__((ext_vector_type(4)));
typedef float f32x2 __attribute__((ext_vector_type(2)));
typedef __bf16 b16x2 __attribute__((ext_vector_type(2)));
typedef unsigned u32x4 __attribute__((ext_vector_type(4)));
typedef unsigned u32x2 __attribute__((ext_vector_type(2)));
#define LAS __attribute__((address_space(3)))
#define DI __device__ __forceinline__

constexpr int PG = 16384;
#define WARM 1
#define WARMGR 128
constexpr size_t WS_O_OFF = 0;
constexpr size_t WS_P_OFF = 8u << 20;
#define O_PERM(d) (((d) & ~31) | ((((d) >> 2) & 3) << 3) | ((((d) >> 4) & 1) << 2) | ((d) & 3))

DI void glds16(unsigned voff, const void* sbase, unsigned lds_dst) {
  unsigned keep;
  asm volatile("s_mov_b32 %0, m0\n\ts_mov_b32 m0, %3\n\ts_nop 0\n\tglobal_load_lds_dwordx4 %1, %2\n\ts_mov_b32 m0, %0"
               : "=&s"(keep) : "v"(voff), "s"(sbase), "s"(lds_dst) : "memory");
}
DI void glds16nt(unsigned voff, const void* sbase, unsigned lds_dst) {
  unsigned keep;
  asm volatile("s_mov_b32 %0, m0\n\ts_mov_b32 m0, %3\n\ts_nop 0\n\tglobal_load_lds_dwordx4 %1, %2 nt\n\ts_mov_b32 m0, %0"
               : "=&s"(keep) : "v"(voff), "s"(sbase), "s"(lds_dst) : "memory");
}
#define NTKV 1
#define NTWQ 0
#define NTX 0
#define WAITV(n) asm volatile("s_waitcnt vmcnt(" #n ")" ::: "memory")
#define BAR() asm volatile("s_waitcnt lgkmcnt(0)\n\ts_barrier" ::: "memory")
DI unsigned pk2(float a, float b) { f32x2 f = {a, b}; return __builtin_bit_cast(unsigned, __builtin_convertvector(f, b16x2)); }
DI bf16x8 ldsr128(LAS char* base, unsigned off) { return *(LAS bf16x8*)(base + off); }
DI bf16x8 ldstr(LAS char* base, unsigned off0, unsigned off1) {
  s16x4 a = __builtin_amdgcn_ds_read_tr16_b64_v4i16((LAS s16x4*)(base + off0));
  s16x4 b = __builtin_amdgcn_ds_read_tr16_b64_v4i16((LAS s16x4*)(base + off1));
  return __builtin_shufflevector(a, b, 0, 1, 2, 3, 4, 5, 6, 7);
}
#define STAMP(i)
#define STAMPDECL
#define STAMPFLUSH
#define STAMP0
#define MFMA32(a, b, c) __builtin_amdgcn_mfma_f32_32x32x16_bf16((a), (b), (c), 0, 0, 0)
#define MFMA16(a, b, c) __builtin_amdgcn_mfma_f32_16x16x32_bf16((a), (b), (c), 0, 0, 0)

#define PSL 2
#define WOK 8
#define CPF 1
#define CPS 128u
#define CPL 10
#define HJ 3
#define HS 3
#include <utility>
#define SB() __builtin_amdgcn_sched_barrier(0)
#define BARN() asm volatile("s_barrier" ::: "memory")
#define PIN(x) asm volatile("" : "+v"(x))
#define PIN2(x) asm volatile("" : "+v"(x) :: "memory")
#define NPA 13
#define NPB 13
#define PSTAMP(i)
#define TSTAMP(i) STAMP(i)
template <int V> struct IC { static constexpr int value = V; };
template <class F, int... Is> DI void static_for_impl(F&& f, std::integer_sequence<int, Is...>) { (f(IC<Is>{}), ...); }
template <int N, class F> DI void static_for(F&& f) { static_for_impl(f, std::make_integer_sequence<int, N>{}); }
DI void gld128(bf16x8& dst, unsigned off, const char* p) { asm volatile("global_load_dwordx4 %0, %1, %2" : "=v"(dst) : "v"(off), "s"(p) : "memory"); }
DI void gtouch(unsigned& dst, unsigned off, const char* p) { asm volatile("global_load_dword %0, %1, %2" : "+v"(dst) : "v"(off), "s"(p) : "memory"); }
DI void gst64_sc1(unsigned off, u32x2 v, const char* p) { asm volatile("global_store_dwordx2 %0, %1, %2 sc1\n\ts_nop 1" :: "v"(off), "v"(v), "s"(p) : "memory"); }
DI void gst64(unsigned off, u32x2 v, const char* p) { asm volatile("global_store_dwordx2 %0, %1, %2\n\ts_nop 1" :: "v"(off), "v"(v), "s"(p) : "memory"); }
DI void gst128(unsigned off, u32x4 v, const char* p) { asm volatile("global_store_dwordx4 %0, %1, %2\n\ts_nop 1" :: "v"(off), "v"(v), "s"(p) : "memory"); }
DI void gst128_sc1(unsigned off, u32x4 v, const char* p) { asm volatile("global_store_dwordx4 %0, %1, %2 sc1\n\ts_nop 1" :: "v"(off), "v"(v), "s"(p) : "memory"); }
DI void gld64_sc1(u32x2& dst, unsigned off, const char* p) { asm volatile("global_load_dwordx2 %0, %1, %2 sc1" : "=v"(dst) : "v"(off), "s"(p) : "memory"); }
DI unsigned QB3(unsigned v) { return (unsigned)__builtin_amdgcn_update_dpp(0, (int)v, 0xFF, 0xF, 0xF, false); }
DI void gld128u(u32x4& dst, unsigned off, const char* p) { asm volatile("global_load_dwordx4 %0, %1, %2" : "=v"(dst) : "v"(off), "s"(p) : "memory"); }
DI void gld128_sc1(u32x4& dst, unsigned off, const char* p) { asm volatile("global_load_dwordx4 %0, %1, %2 sc1" : "=v"(dst) : "v"(off), "s"(p) : "memory"); }
extern "C" __device__ unsigned long long hw_dispatch_id() __asm("llvm.amdgcn.dispatch.id");
constexpr unsigned OBY = 32768;
constexpr unsigned XB = 36864;
constexpr size_t WS_O2_OFF = 0;
constexpr size_t WS_S2_OFF = 16u << 20;
constexpr unsigned WU = 9216;
constexpr unsigned wpos(int L) { return L < 3 ? 131072u - WU * (L + 1) : L < 6 ? WU * (L - 3) : L < 9 ? 131072u - WU * (L - 2) : L < 13 ? WU * (L - 6) : 131072u + WU * (L - 13); }
constexpr unsigned SPIN_MAX = 1u << 16;
#define OPS 2, 3, 4, 5, 6, 7,  8, 9, 10, 11,  12, 14, 16, 18, 20, 22,  24, 25, 26, 27,  28, 30, 32, 34, 36, 38
constexpr int opStepT[26] = {OPS};
constexpr int opStep(int j) { return opStepT[j]; }
constexpr int opsBefore(int i) { int n = 0; for (int j = 0; j < 26; ++j) if (opStepT[j] < i) ++n; return n; }
#define WAITN(n) asm volatile("s_waitcnt vmcnt(%0)" :: "n"(n) : "memory")

__global__ __launch_bounds__(512, 2) void k_fused(const u16* __restrict__ x, const u16* __restrict__ kc, const u16* __restrict__ vc,
                                                  const u16* __restrict__ wq, const u16* __restrict__ wo, char* __restrict__ ws, u16* __restrict__ out, unsigned long long* stamps) {
  STAMPDECL;
  unsigned wt = 0;
  {
    unsigned long long pc;
    unsigned clen;
    asm volatile("s_getpc_b64 %0\n.Lkf_code_begin:\n\ts_mov_b32 %1, kf_code_end-.Lkf_code_begin-4" : "=s"(pc), "=s"(clen));
    const unsigned l_ = threadIdx.x;
    unsigned co = ((blockIdx.x >> 3) * CPL + (l_ < CPL ? l_ : CPL - 1)) * CPS;
    co = co < clen ? co : clen;
    if (l_ < 64) asm volatile("global_load_dword %0, %1, %2" : "+v"(wt) : "v"(co), "s"(pc) : "memory");
  }
  extern __shared__ __attribute__((aligned(16))) char smem_[];
  LAS char* smem = (LAS char*)smem_;
  const unsigned lds0 = (unsigned)(size_t)smem;
  const int tid = threadIdx.x, lane = tid & 63;
  const int w = __builtin_amdgcn_readfirstlane(tid >> 6);
  const int bid = blockIdx.x, xcd = bid & 7, jj = bid >> 3;
  const int h = xcd * 4 + (jj & 3), b = jj >> 2;
  const int hh = lane >> 5, l31 = lane & 31, dsub = (lane >> 4) & 1, qq = (lane & 15) >> 2, p3 = lane & 3;
  const char* xb = (const char*)x + (size_t)b * 112 * 1152;
  const char* wqh = (const char*)wq + h * 256;
  const char* kb = (const char*)kc + (size_t)(b * 16 + (h >> 1)) * 131072;
  const char* vb = (const char*)vc + (size_t)(b * 16 + (h >> 1)) * 131072;
  char* Ows = ws + WS_O2_OFF;
  char* part = ws + WS_S2_OFF;
  unsigned tag;
  {
    const unsigned long long did = hw_dispatch_id();
    const unsigned long long dp = (unsigned long long)__builtin_amdgcn_dispatch_ptr();
    tag = ((unsigned)did * 0x9E3779B1u) ^ (unsigned)(dp >> 6) ^ ((unsigned)(did >> 32) * 0x85EBCA6Bu) ^ 0xA5C3F00Du;
  }
  const int wl = w & 3;
  constexpr unsigned SL = 32768;
  const int ks = xcd, nq = jj & 3;
  auto issueWo = [&](int L, unsigned pos) {
    const char* src = (const char*)wo + ((size_t)(ks * 512 + ((nq + (L >> 2)) & 3) * 128 + (L & 3) * 32) * 576 + nq * 144) * 2;
#pragma unroll
    for (int p = 0; p < 9; ++p) {
      const int c = p * 64 + lane;
      glds16((unsigned)((c / 18) * 1152 + (c % 18) * 16), src, lds0 + pos + p * 1024);
    }
  };
  const unsigned voffV = (unsigned)((lane >> 4) * 256 + (((lane & 15) ^ ((lane >> 4) << 2)) << 4));
  auto issueKV = [&](const char* base, bool isK, int key0, unsigned slot) {
#pragma unroll
    for (int pp = 0; pp < 8; ++pp) {
      const int p = wl + 4 * pp;
      unsigned off;
      if (isK) off = (unsigned)((lane >> 4) * 256 + (((lane & 15) ^ ((4 * p + (lane >> 4)) & 15)) << 4));
      else off = voffV;
      glds16nt(off, base + (size_t)(key0 + 4 * p) * 256, lds0 + slot * SL + p * 1024);
    }
  };
  const int qg = w & 3;
  const unsigned aR = (unsigned)(l31 * 256 + ((hh ^ (l31 & 15)) << 4));
  const float cs = 0.08838834764831845f * 1.4426950408889634f;

  auto tail = [&](auto Gc) {
    constexpr int G = decltype(Gc)::value;
    constexpr int NP = G == 0 ? NPA : NPB;
    constexpr unsigned KS0 = G == 0 ? 4 : 2, KS1 = G == 0 ? 0 : 3, VS0 = G == 0 ? 1 : 0, VS1 = G == 0 ? 4 : 2;
    constexpr int ST0 = 12;
    bf16x8 qf[8];
#pragma unroll
    for (int s = 0; s < 8; ++s) qf[s] = ldsr128(smem, 1 * SL + qg * 8192 + (aR ^ (s << 5)));
    if (G == 1) {
      WAITV(16); BAR();
      STAMP(ST0);
    }
    f32x16 S[8];
    float mx = -3.0e38f;
    constexpr int PD = 4, NB = 6, E = 4;
    bf16x8 kf[NB];
    constexpr unsigned KW0 = (KS0 >> 1) * 65536u, KW1 = (KS1 >> 1) * 65536u;
    constexpr unsigned KO0 = (KS0 & 1) * SL, KO1 = (KS1 & 1) * SL;
    unsigned kA[8], kB[8];
#pragma unroll
    for (int s = 0; s < 8; ++s) { kA[s] = KW0 + (aR ^ (s << 5)); PIN(kA[s]); if (KW1 != KW0) { kB[s] = KW1 + (aR ^ (s << 5)); PIN(kB[s]); } else kB[s] = kA[s]; }
    auto kread = [&](int i) -> bf16x8 {
      const int t = i >> 3, s = i & 7;
      return t < 4 ? ldsr128(smem, kA[s] + (KO0 + t * 8192)) : ldsr128(smem, kB[s] + (KO1 + (t - 4) * 8192));
    };
    if (G == 1) {
      issueKV(vb, false, 0, 1);
      WAITV(16); BARN();
      STAMP(ST0 + 1);
    }
    __builtin_amdgcn_s_setprio(1);
#pragma unroll
    for (int i = 0; i < PD; ++i) kf[i % NB] = kread(i);
    SB();
#pragma unroll
    for (int i = 0; i < 64; ++i) {
      const int t = i >> 3, s = i & 7;
      const int ip = i + PD;
      if (ip == 32) { if (G == 1) WAITV(8); BARN(); STAMP(G == 0 ? ST0 : ST0 + 2); }
      if (i == 64 - E) { if (G == 1) WAITV(0); BARN(); STAMP(G == 0 ? ST0 + 1 : ST0 + 3); }
      if (s == 0) { f32x16 z = {}; S[t] = MFMA32(kf[i % NB], qf[s], z); }
      else S[t] = MFMA32(kf[i % NB], qf[s], S[t]);
      SB();
      if (ip < 64) kf[ip % NB] = kread(ip);
      if (t > 0) mx = fmaxf(fmaxf(mx, S[t - 1][2 * s]), S[t - 1][2 * s + 1]);
      SB();
    }
    __builtin_amdgcn_s_setprio(0);
    const unsigned wto = (unsigned)((128 * h + 16 * b) * 1152 + (wl * 36 + (lane < 36 ? lane : 35)) * 128);
    if (G == 0) {
      issueKV(vb, false, 128, 4);
      if (WO2 == 0) gtouch(wt, wto, (const char*)wo);
    } else issueKV(vb, false, 384, 2);
#pragma unroll
    for (int r = 0; r < 16; r += 2) mx = fmaxf(fmaxf(mx, S[7][r]), S[7][r + 1]);
    mx = fmaxf(mx, __shfl_xor(mx, 32));
    const float nmc = -mx * cs;
    bf16x8 P[16];
    auto mkP = [&](int ks) -> bf16x8 {
      const int t = ks >> 1, s = ks & 1;
      float p[8];
#pragma unroll
      for (int j = 0; j < 8; ++j) p[j] = __builtin_amdgcn_exp2f(fmaf(S[t][8 * s + j], cs, nmc));
      u32x4 pw = {pk2(p[0], p[1]), pk2(p[2], p[3]), pk2(p[4], p[5]), pk2(p[6], p[7])};
      return __builtin_bit_cast(bf16x8, pw);
    };
#pragma unroll
    for (int ks = 0; ks < NP; ++ks) {
      if (ks == NP / 2) {
        SB();
        BARN(); STAMP(G == 0 ? ST0 + 2 : ST0 + 4);
        if (G == 0) issueKV(vb, false, 256, 0);
        SB();
      }
      P[ks] = mkP(ks);
    }
    SB();
    BARN(); STAMP(G == 0 ? ST0 + 3 : ST0 + 5);
    if (G == 0 && (WO2 == 1 || WO2 == 7)) gtouch(wt, wto, (const char*)wo);
    __builtin_amdgcn_s_setprio(1);
    f32x16 O[4] = {};
    f32x4 lacc = {};
    const unsigned onev = (((lane >> 4) & 1) == (lane & 1)) ? 0x3f803f80u : 0u;
    const u32x4 ones4 = {onev, onev, onev, onev};
    const bf16x8 onesA = __builtin_bit_cast(bf16x8, ones4);
    unsigned aV0[4], aV1[4];
    constexpr unsigned VW0 = (VS0 >> 1) * 65536u, VW1 = (VS1 >> 1) * 65536u, VO0 = (VS0 & 1) * SL, VO1 = (VS1 & 1) * SL;
#pragma unroll
    for (int dt = 0; dt < 4; ++dt) {
      const unsigned a = (unsigned)((4 * hh + qq) * 256 + 64 * (dt ^ qq) + 32 * dsub + 8 * p3);
      aV0[dt] = a + VW0; PIN(aV0[dt]); aV1[dt] = a + VW1; PIN(aV1[dt]);
    }
    auto vread = [&](int ks, int dt) -> bf16x8 {
      const unsigned a = ks < 8 ? aV0[dt] + (VO0 + ks * 4096) : aV1[dt] + (VO1 + (ks - 8) * 4096);
      return ldstr(smem, a, a + 2048);
    };
    bf16x8 vf[2][4];
#pragma unroll
    for (int dt = 0; dt < 4; ++dt) vf[0][dt] = vread(0, dt);
    SB();
    constexpr int NE = (16 - NP) * 8;
    float tt[NE + 1], ee[NE + 1];
    unsigned pk[NE / 2 + 1];
#pragma unroll
    for (int ks = 0; ks < 16; ++ks) {
      const int cur = ks & 1, nx = cur ^ 1;
      if (ks == 7) {
        if (G == 0) { if (WO2 >= 2 && WO2 != 7) WAITV(8); else WAITV(9); } else WAITV(0);
        BARN(); STAMP(G == 0 ? ST0 + 4 : ST0 + 6);
        if (G == 0 && WO2 == 2) gtouch(wt, wto, (const char*)wo);
      }
      if (G == 0 && WO2 == 6 && ks == WOK) gtouch(wt, wto, (const char*)wo);
      if (G == 0 && ks == 15) { if (WO2 == 6 || WO2 == 7) WAITV(1); else WAITV(0); BAR(); STAMP(ST0 + 5); }
#pragma unroll
      for (int dt = 0; dt < 4; ++dt) {
        const int j = ks * 4 + dt;
        O[dt] = MFMA32(vf[cur][dt], P[ks], O[dt]);
        SB();
        if (ks + 1 < 16) vf[nx][dt] = vread(ks + 1, dt);
        if (j < NE) { const int e = NP * 8 + j; tt[j] = fmaf(S[e >> 4][e & 15], cs, nmc); }
        if (j >= 1 && j - 1 < NE) ee[j - 1] = __builtin_amdgcn_exp2f(tt[j - 1]);
        if (j >= 3 && j - 2 < NE && ((j - 2) & 1)) {
          const int pi = (j - 3) >> 1;
          pk[pi] = pk2(ee[j - 3], ee[j - 2]);
          if ((pi & 3) == 3) { u32x4 pw = {pk[pi - 3], pk[pi - 2], pk[pi - 1], pk[pi]}; P[NP + (pi >> 2)] = __builtin_bit_cast(bf16x8, pw); }
        }
        SB();
      }
      lacc = MFMA16(onesA, P[ks], lacc);
      SB();
    }
    __builtin_amdgcn_s_setprio(0);
    if (G == 0 && WO2 == 3) gtouch(wt, wto, (const char*)wo);
    const float lsum = (lane & 16) ? lacc[1] : lacc[0];
    STAMP(20);
    const unsigned xar = qg < 2 ? 1u * SL + qg * 16384 : 4u * SL + (qg & 1) * 16384;
    const unsigned xst = qg < 3 ? 1024u : 512u;
    const unsigned xlo = qg < 3 ? (unsigned)lane * 16 : (unsigned)((hh << 4) | (lane & 15)) * 16;
    const int row = 32 * qg + l31;
    constexpr unsigned STATS = 4 * SL + 24576;
    if (G == 0) {
      if (row < 112) {
#pragma unroll
        for (int dt = 0; dt < 4; ++dt)
#pragma unroll
          for (int g = 0; g < 4; ++g) {
            f32x4 v = {O[dt][4 * g], O[dt][4 * g + 1], O[dt][4 * g + 2], O[dt][4 * g + 3]};
            *(LAS f32x4*)(smem + xar + (dt * 4 + g) * xst + xlo) = v;
          }
      }
      f32x2 st = {mx, lsum};
      *(LAS f32x2*)(smem + STATS + qg * 512 + lane * 8) = st;
      BAR();
      STAMP(ST0 + 6);
      if (WO2 == 5) gtouch(wt, wto, (const char*)wo);
      asm volatile("s_waitcnt vmcnt(0)" : "+v"(wt) :: "memory");
      if (wl < 3) { issueWo(wl, 131072u - WU * (wl + 1)); issueWo(3 + wl, WU * wl); }
      BARN();
      if (wl < 3) issueWo(6 + wl, 131072u - WU * (wl + 4));
    } else {
      f32x2 st = *(LAS f32x2*)(smem + STATS + qg * 512 + lane * 8);
      f32x4 xv[16];
#pragma unroll
      for (int i = 0; i < 16; ++i) xv[i] = *(LAS f32x4*)(smem + xar + i * xst + xlo);
      const float m2 = fmaxf(mx, st[0]);
      const float a1 = __builtin_amdgcn_exp2f((mx - m2) * cs), a2 = __builtin_amdgcn_exp2f((st[0] - m2) * cs);
      const float inv = __builtin_amdgcn_rcpf(a1 * lsum + a2 * st[1]);
      const float f1 = a1 * inv, f2 = a2 * inv;
      const char* og = Ows + (size_t)bid * OBY;
      const unsigned oo = (unsigned)((row >> 4) * 1024 + hh * 256 + (row & 15) * 16);
      const unsigned ov = (unsigned)(28672 + (((row >> 4) * 2 + hh) * 4 + ((row & 15) >> 2)) * 32 + (lane & 3) * 8);
      const unsigned ol = xar + (unsigned)((l31 >> 4) * 1024 + hh * 256 + (row & 15) * 16);
      const int m4 = lane & 3;
      asm volatile("s_waitcnt lgkmcnt(0)" ::: "memory");
#pragma unroll
      for (int kh2 = 0; kh2 < 2; ++kh2) {
        unsigned F[2][2][4];
#pragma unroll
        for (int d1 = 0; d1 < 2; ++d1) {
          const int dt = 2 * kh2 + d1;
          float r[16];
#pragma unroll
          for (int g = 0; g < 4; ++g)
#pragma unroll
            for (int e = 0; e < 4; ++e) r[4 * g + e] = O[dt][4 * g + e] * f1 + xv[dt * 4 + g][e] * f2;
#pragma unroll
          for (int g1 = 0; g1 < 2; ++g1) {
            F[d1][g1][0] = pk2(r[4 * g1], r[4 * g1 + 1]); F[d1][g1][1] = pk2(r[4 * g1 + 2], r[4 * g1 + 3]);
            F[d1][g1][2] = pk2(r[4 * (g1 + 2)], r[4 * (g1 + 2) + 1]); F[d1][g1][3] = pk2(r[4 * (g1 + 2) + 2], r[4 * (g1 + 2) + 3]);
          }
        }
        const unsigned z00 = QB3(F[0][0][3]), z01 = QB3(F[0][1][3]), z10 = QB3(F[1][0][3]), z11 = QB3(F[1][1][3]);
        const u32x2 zv = {m4 == 0 ? z00 : m4 == 1 ? z01 : 0u, m4 == 0 ? z10 : m4 == 1 ? z11 : m4 == 3 ? tag : 0u};
        if (row < 112) {
#pragma unroll
          for (int d1 = 0; d1 < 2; ++d1)
#pragma unroll
            for (int g1 = 0; g1 < 2; ++g1) {
              u32x4 u = {F[d1][g1][0], F[d1][g1][1], F[d1][g1][2], m4 == 3 ? tag : F[d1][g1][3]};
              gst128_sc1(oo, u, og + (2 * kh2 + d1) * 7168 + g1 * 512);
            }
          gst64_sc1(ov, zv, og + kh2 * 1792);
#pragma unroll
          for (int d1 = 0; d1 < 2; ++d1)
#pragma unroll
            for (int g1 = 0; g1 < 2; ++g1) {
              u32x4 v = {F[d1][g1][0], F[d1][g1][1], F[d1][g1][2], F[d1][g1][3]};
              *(LAS u32x4*)(smem + ol + (2 * kh2 + d1) * 2048 + g1 * 512) = v;
            }
        }
      }
      BAR();
    }
  };

  unsigned xoff[4];
#pragma unroll
  for (int pp = 0; pp < 4; ++pp) {
    int p = wl + 4 * pp; int row = 8 * p + (lane >> 3); if (row > 111) row = 111;
    int ch = (lane & 7) ^ ((4 * (p & 1) + (lane >> 4)) & 7);
    xoff[pp] = (unsigned)(row * 1152 + ch * 16);
  }
  const unsigned woff = (unsigned)((lane >> 4) * 8192 + (((lane & 15) ^ ((lane >> 4) << 2)) * 16));
  auto issueW = [&](int c) {
    const unsigned sb = lds0 + (unsigned)(c % 5) * SL;
#pragma unroll
    for (int pp = 0; pp < 4; ++pp) { glds16(woff, wqh + (size_t)(64 * c + 4 * (wl + 4 * pp)) * 8192, sb + 16384 + (wl + 4 * pp) * 1024); }
#pragma unroll
    for (int pp = 0; pp < 4; ++pp) if (pp < 3 || wl < 2) { glds16(xoff[pp], xb + c * 128, sb + (wl + 4 * pp) * 1024); }
  };
  if (w >= 4) {
    issueW(0); issueW(1);
    BARN();
#pragma unroll
    for (int c = 0; c < 9; ++c) {
      if (wl < 2) {
        switch (c) { case 0: WAITV(8); break; case 1: WAITV(0); break; case 2: WAITV(16); break; case 3: case 4: case 5: case 6: case 7: WAITV(24); break; default: WAITV(16); break; }
      } else {
        switch (c) { case 0: WAITV(7); break; case 1: WAITV(0); break; case 2: WAITV(14); break; case 3: case 4: case 5: WAITV(21); break; case 6: WAITV(22); break; case 7: WAITV(23); break; default: WAITV(16); break; }
      }
      BARN();
      PSTAMP(2 + c);
      if (c == 1) { issueW(2); issueW(3); issueW(4); }
      else if (c == 2) { issueW(5); issueW(6); }
      else if (c == 3) issueW(7);
      else if (c == 4) issueW(8);
      else if (c == 5) issueKV(kb, true, 0, 4);
      else if (c == 6) issueKV(kb, true, 128, 0);
    }
    WAITV(8);
    BARN();
    STAMP(11);
    issueKV(kb, true, 256, 2); issueKV(kb, true, 384, 3);
    tail(IC<1>{});
  } else {
#define tch wt
    {
      const unsigned lo = (unsigned)lane;
      const unsigned long long tpb = lane < 16 ? (unsigned long long)wqh : (unsigned long long)xb;
      const unsigned tl = lane < 44 ? lo : 43;
      const unsigned tpo = lane < 16 ? (8 * b + (tl >> 1)) * 8192 + (tl & 1) * 128 : (28 * (h & 3) + (tl - 16)) * 1152;
      const unsigned tps = lane < 16 ? 64 * 8192 : 128;
      const char* tpa = (const char*)(tpb + tpo) + (size_t)(2 + wl) * tps;
      const char* tpa2 = tpa + (size_t)4 * tps;
      BARN();
      asm volatile("global_load_dword %0, %1, off" : "+v"(tch) : "v"(tpa) : "memory");
      if (wl < 3) asm volatile("global_load_dword %0, %1, off" : "+v"(tch) : "v"(tpa2) : "memory");
    }
    const int dp = w & 1, qp = w >> 1;
    const unsigned aW0 = (unsigned)(16384 + (8 * hh + qq) * 256 + 64 * ((2 * dp) ^ qq) + 32 * dsub + 8 * p3);
    const unsigned aW1 = (unsigned)(16384 + (8 * hh + qq) * 256 + 64 * ((2 * dp + 1) ^ qq) + 32 * dsub + 8 * p3);
    const unsigned aX = (unsigned)((64 * qp + l31) * 128 + ((hh ^ ((l31 >> 1) & 7)) << 4));
    f32x16 accq[2][2] = {};
    bf16x8 A0[3], A1[3], B0[3], B1[3];
    unsigned aWa[3], aWb[3], aXs[3][4];
#pragma unroll
    for (int wdw = 0; wdw < 3; ++wdw) {
      aWa[wdw] = aW0 + wdw * 65536u; PIN(aWa[wdw]); aWb[wdw] = aW1 + wdw * 65536u; PIN(aWb[wdw]);
#pragma unroll
      for (int s = 0; s < 4; ++s) { aXs[wdw][s] = (aX ^ (s << 5)) + wdw * 65536u; PIN(aXs[wdw][s]); }
    }
    auto rdA = [&](int st, int which) -> bf16x8 {
      const int sl = (st >> 2) % 5, s1 = st & 3; const unsigned o = (sl & 1) * SL + s1 * 4096;
      const unsigned a = (which ? aWb[sl >> 1] : aWa[sl >> 1]) + o;
      return ldstr(smem, a, a + 1024);
    };
    auto rdB = [&](int st, int u) -> bf16x8 {
      const int sl = (st >> 2) % 5, s1 = st & 3;
      return ldsr128(smem, aXs[sl >> 1][s1] + ((sl & 1) * SL + u * 4096));
    };
    BARN();
    PSTAMP(2);
#pragma unroll
    for (int i = 0; i < 2; ++i) { A0[i] = rdA(i, 0); B0[i] = rdB(i, 0); B1[i] = rdB(i, 1); A1[i] = rdA(i, 1); }
    SB();
    __builtin_amdgcn_s_setprio(1);
#pragma unroll
    for (int i = 0; i < 36; ++i) {
      const int cur = i % 3, ip = i + 2, nx = ip % 3;
      if (ip < 36 && (ip & 3) == 0) { BAR(); PSTAMP(2 + (ip >> 2)); }
      accq[0][0] = MFMA32(A0[cur], B0[cur], accq[0][0]);
      SB();
      if (ip < 36) A0[nx] = rdA(ip, 0);
      SB();
      accq[0][1] = MFMA32(A0[cur], B1[cur], accq[0][1]);
      SB();
      if (ip < 36) { B0[nx] = rdB(ip, 0); B1[nx] = rdB(ip, 1); }
      SB();
      accq[1][0] = MFMA32(A1[cur], B0[cur], accq[1][0]);
      SB();
      if (ip < 36) A1[nx] = rdA(ip, 1);
      SB();
      accq[1][1] = MFMA32(A1[cur], B1[cur], accq[1][1]);
      SB();
    }
    __builtin_amdgcn_s_setprio(0);
#pragma unroll
    for (int a = 0; a < 2; ++a)
#pragma unroll
      for (int u = 0; u < 2; ++u) {
        const int row = 64 * qp + 32 * u + l31;
#pragma unroll
        for (int g = 0; g < 4; ++g) {
          u32x2 v = {pk2(accq[a][u][4 * g], accq[a][u][4 * g + 1]), pk2(accq[a][u][4 * g + 2], accq[a][u][4 * g + 3])};
          if (row >= 112) { v[0] = 0; v[1] = 0; }
          *(LAS u32x2*)(smem + 1 * SL + row * 256 + (((4 * (2 * dp + a) + g) ^ (row & 15)) << 4) + 8 * hh) = v;
        }
      }
    asm volatile("s_waitcnt vmcnt(0)" : "+v"(tch) :: "memory");
    BAR();
    STAMP(11);
    tail(IC<0>{});
  }

  TSTAMP(1);
  const int rp = w & 3, kh = w >> 2;
  const int l15 = lane & 15, g = lane >> 4, m4 = lane & 3;
  const bool q3 = m4 == 3;
  bf16x8 Bf[4][2][2];
  {
    const unsigned own = (rp < 2 ? 1u * SL + rp * 16384 : 4u * SL + (rp & 1) * 16384) + kh * 4096 + lane * 16;
#pragma unroll
    for (int j = 0; j < 2; ++j) { Bf[0][j][0] = ldsr128(smem, own + j * 2048); Bf[0][j][1] = ldsr128(smem, own + j * 2048 + (rp < 3 ? 1024 : 0)); }
  }
  if (w < 3) WAITV(9);
  BAR();
  TSTAMP(2);
  const int rt0 = 2 * rp, rt1 = rp == 3 ? 6 : 2 * rp + 1;
  const unsigned o0 = (unsigned)(kh * 14336 + rt0 * 1024 + lane * 16), o1 = (unsigned)(kh * 14336 + rt1 * 1024 + lane * 16);
  const unsigned vsl = (unsigned)(28672 + kh * 1792 + ((g & 1) * 4 + (l15 >> 2)) * 32 + (q3 ? (g >> 1) * 8 : 24));
  const unsigned v0o = vsl + rt0 * 256, v1o = vsl + rt1 * 256;
  u32x4 U[3][2][2];
  u32x2 V[3][2];
  const char* Ob[3];
#pragma unroll
  for (int t = 0; t < 3; ++t) Ob[t] = Ows + (size_t)((b * 4 + ((nq + 1 + t) & 3)) * 8 + ks) * OBY;
  unsigned wqo[8], wql[8];
#pragma unroll
  for (int j = 0; j < 8; ++j) {
    int q = w + 8 * j; if (j == 7 && q > 62) q = 62;
    const int Lq = (q * 57) >> 9, p = q - 9 * Lq, L = 9 + Lq;
    const int c = p * 64 + lane, cr = (c * 3641) >> 16;
    wqo[j] = (unsigned)((((nq + (L >> 2)) & 3) * 128 + (L & 3) * 32 + cr) * 1152 + (c - cr * 18) * 16);
    wql[j] = lds0 + (L < 13 ? WU * (unsigned)(L - 6) : 131072u + WU * (unsigned)(L - 13)) + (unsigned)p * 1024u;
  }
  const char* wob = (const char*)wo + ((size_t)ks * 512 * 576 + nq * 144) * 2;
  auto memop = [&](auto Jc) __attribute__((always_inline)) {
    constexpr int j = decltype(Jc)::value;
    if constexpr (j >= 6 && j < 10) glds16(wqo[j - 6], wob, wql[j - 6]);
    else if constexpr (j >= 16 && j < 20) glds16(wqo[j - 12], wob, wql[j - 12]);
    else {
      constexpr int t = j < 6 ? 0 : j < 16 ? 1 : 2, e = j < 6 ? j : j < 16 ? j - 10 : j - 20;
      if constexpr (e == 0) gld128_sc1(U[t][0][0], o0, Ob[t]);
      else if constexpr (e == 1) gld128_sc1(U[t][1][0], o1, Ob[t]);
      else if constexpr (e == 2) gld128_sc1(U[t][0][1], o0, Ob[t] + 7168);
      else if constexpr (e == 3) gld128_sc1(U[t][1][1], o1, Ob[t] + 7168);
      else if constexpr (e == 4) gld64_sc1(V[t][0], v0o, Ob[t]);
      else gld64_sc1(V[t][1], v1o, Ob[t]);
    }
  };
  f32x4 acc[9][2] = {};
  const unsigned aWl = (unsigned)((4 * g + qq) * 288 + 8 * p3);
  unsigned aWs[8];
  static_for<8>([&](auto Ic) __attribute__((always_inline)) {
    constexpr int i = decltype(Ic)::value;
    aWs[i] = aWl + (kh ? wpos(4 * (i >> 1) + 2 + (i & 1)) : wpos(4 * (i >> 1) + (i & 1))); PIN(aWs[i]);
  });
  constexpr int PD = 4, NB = 6, NT = 72;
  bf16x8 Af[NB];
  auto rdA = [&](int i) -> bf16x8 {
    const int kstep = i / 9, c = i % 9;
    return ldstr(smem, aWs[kstep] + c * 32, aWs[kstep] + (c * 32 + 16 * 288));
  };
  auto chkU = [&](int t) {
    asm volatile("" : "+v"(U[t][0][0]), "+v"(U[t][1][0]), "+v"(U[t][0][1]), "+v"(U[t][1][1]), "+v"(V[t][0]), "+v"(V[t][1]) :: "memory");
    for (unsigned spins = 0; spins < SPIN_MAX; ++spins) {
      const bool ok = q3 ? (U[t][0][0][3] == tag && U[t][1][0][3] == tag && U[t][0][1][3] == tag && U[t][1][1][3] == tag) : (V[t][0][1] == tag && V[t][1][1] == tag);
      if (__all(ok)) break;
      __builtin_amdgcn_s_sleep(2);
      asm volatile("global_load_dwordx4 %0, %6, %10 sc1\n\tglobal_load_dwordx4 %1, %7, %10 sc1\n\t"
                   "global_load_dwordx4 %2, %6, %11 sc1\n\tglobal_load_dwordx4 %3, %7, %11 sc1\n\t"
                   "global_load_dwordx2 %4, %8, %10 sc1\n\tglobal_load_dwordx2 %5, %9, %10 sc1\n\ts_waitcnt vmcnt(0)"
                   : "=&v"(U[t][0][0]), "=&v"(U[t][1][0]), "=&v"(U[t][0][1]), "=&v"(U[t][1][1]), "=&v"(V[t][0]), "=&v"(V[t][1])
                   : "v"(o0), "v"(o1), "v"(v0o), "v"(v1o), "s"(Ob[t]), "s"(Ob[t] + 7168) : "memory");
    }
#pragma unroll
    for (int r = 0; r < 2; ++r)
#pragma unroll
      for (int j = 0; j < 2; ++j) {
        u32x4 f = {U[t][r][j][0], U[t][r][j][1], U[t][r][j][2], q3 ? V[t][r][j] : U[t][r][j][3]};
        Bf[t + 1][j][r] = __builtin_bit_cast(bf16x8, f);
      }
  };
#pragma unroll
  for (int i = 0; i < PD; ++i) Af[i % NB] = rdA(i);
  SB();
  static_for<NT>([&](auto Ic) __attribute__((always_inline)) {
    constexpr int i = decltype(Ic)::value;
    constexpr int kstep = i / 9, c = i % 9, ip = i + PD;
    if constexpr (i == 18 - PD) { if (w < 3) WAITN(opsBefore(i)); BARN(); TSTAMP(3); }
    if constexpr (i == 18) { WAITN(opsBefore(i) - 6); chkU(0); TSTAMP(4); SB(); }
    if constexpr (i == 36 - PD) { WAITN(opsBefore(i) - 10); BARN(); TSTAMP(23); }
    if constexpr (i == 36) { WAITN(opsBefore(i) - 16); chkU(1); TSTAMP(24); SB(); }
    if constexpr (i == 54 - PD) { WAITN(opsBefore(i) - 20); BARN(); TSTAMP(25); }
    if constexpr (i == 54) { WAITV(0); chkU(2); TSTAMP(26); SB(); }
    static_for<26>([&](auto Jc) __attribute__((always_inline)) {
      if constexpr (opStep(decltype(Jc)::value) == i) { memop(Jc); SB(); }
    });
    acc[c][0] = MFMA16(Af[i % NB], Bf[kstep >> 1][kstep & 1][0], acc[c][0]);
    SB();
    if constexpr (ip < NT) Af[ip % NB] = rdA(ip);
    SB();
    acc[c][1] = MFMA16(Af[i % NB], Bf[kstep >> 1][kstep & 1][1], acc[c][1]);
    SB();
  });
  TSTAMP(5);
  const unsigned xbw = w < 5 ? (unsigned)w * 10240u : 65536u + (unsigned)(w - 5) * 10240u;
  const unsigned xbr = (w ^ 4) < 5 ? (unsigned)(w ^ 4) * 10240u : 65536u + (unsigned)((w ^ 4) - 5) * 10240u;
  LAS char* xw = smem + xbw + lane * 16;
  LAS char* xr = smem + xbr + lane * 16;
  LAS char* xw2 = smem + xbw + 8192 + lane * 8;
  LAS char* xr2 = smem + xbr + 8192 + lane * 8;
  if (kh) {
#pragma unroll
    for (int c = 0; c < 4; ++c) { *(LAS f32x4*)(xw + (2 * c) * 1024) = acc[c][0]; *(LAS f32x4*)(xw + (2 * c + 1) * 1024) = acc[c][1]; }
#pragma unroll
    for (int r = 0; r < 2; ++r) { f32x2 v = {acc[4][r][0], acc[4][r][1]}; *(LAS f32x2*)(xw2 + r * 512) = v; }
  } else {
#pragma unroll
    for (int c = 0; c < 4; ++c) { *(LAS f32x4*)(xw + (2 * c) * 1024) = acc[5 + c][0]; *(LAS f32x4*)(xw + (2 * c + 1) * 1024) = acc[5 + c][1]; }
#pragma unroll
    for (int r = 0; r < 2; ++r) { f32x2 v = {acc[4][r][2], acc[4][r][3]}; *(LAS f32x2*)(xw2 + r * 512) = v; }
  }
  BAR();
  TSTAMP(6);
  char* slabs = part + (size_t)(b * 4 + nq) * 8 * XB;
  {
    unsigned E[18];
#pragma unroll
    for (int r = 0; r < 2; ++r) {
      const f32x2 h4 = *(LAS f32x2*)(xr2 + r * 512);
      if (kh == 0) {
#pragma unroll
        for (int c = 0; c < 4; ++c) { const f32x4 t = acc[c][r] + *(LAS f32x4*)(xr + (2 * c + r) * 1024); E[9 * r + 2 * c] = pk2(t[0], t[1]); E[9 * r + 2 * c + 1] = pk2(t[2], t[3]); }
        E[9 * r + 8] = pk2(acc[4][r][0] + h4[0], acc[4][r][1] + h4[1]);
      } else {
        E[9 * r] = pk2(h4[0] + acc[4][r][2], h4[1] + acc[4][r][3]);
#pragma unroll
        for (int c = 0; c < 4; ++c) { const f32x4 t = *(LAS f32x4*)(xr + (2 * c + r) * 1024) + acc[5 + c][r]; E[9 * r + 1 + 2 * c] = pk2(t[0], t[1]); E[9 * r + 2 + 2 * c] = pk2(t[2], t[3]); }
      }
    }
    const unsigned x0 = QB3(E[3]), x1 = QB3(E[7]), x2 = QB3(E[11]), x3 = QB3(E[15]);
    const char* slab = slabs + (size_t)ks * XB + (rp < 3 ? (rp * 2 + kh) * 5120 : 30720 + kh * 3072);
    const unsigned so = (unsigned)lane * 16;
#pragma unroll
    for (int s = 0; s < 2; ++s) { u32x4 v = {E[4 * s], E[4 * s + 1], E[4 * s + 2], q3 ? tag : E[4 * s + 3]}; gst128_sc1(so, v, slab + s * 1024); }
    if (rp < 3) {
#pragma unroll
      for (int s = 2; s < 4; ++s) { u32x4 v = {E[4 * s], E[4 * s + 1], E[4 * s + 2], q3 ? tag : E[4 * s + 3]}; gst128_sc1(so, v, slab + s * 1024); }
      u32x4 v = {E[16], E[17], m4 == 0 ? x0 : m4 == 1 ? x1 : m4 == 2 ? x2 : x3, q3 ? tag : 0u};
      gst128_sc1(so, v, slab + 4096);
    } else {
      u32x4 v = {E[8], m4 == 0 ? x0 : x1, 0u, q3 ? tag : 0u};
      gst128_sc1(so, v, slab + 2048);
    }
  }
  TSTAMP(7);
  const int u = ks + 8 * w;
  if (u < 36) {
    u32x4 v[8];
    const unsigned off = (unsigned)(u * 1024 + lane * 16);
    unsigned need = 0xffu;
    __builtin_amdgcn_s_sleep(RSLEEP);
    for (unsigned spins = 0; spins < SPIN_MAX; ++spins) {
#define RL(k, pk) "s_bitcmp1_b32 %9, " #k "\n\ts_cbranch_scc0 .Lrl%=_" #k "\n\tglobal_load_dwordx4 %" #k ", %8, %" #pk " sc1\n.Lrl%=_" #k ":\n\t"
      asm volatile(RL(0, 10) RL(1, 11) RL(2, 12) RL(3, 13) RL(4, 14) RL(5, 15) RL(6, 16) RL(7, 17) "s_waitcnt vmcnt(0)"
                   : "+v"(v[0]), "+v"(v[1]), "+v"(v[2]), "+v"(v[3]), "+v"(v[4]), "+v"(v[5]), "+v"(v[6]), "+v"(v[7])
                   : "v"(off), "s"(need), "s"(slabs), "s"(slabs + XB), "s"(slabs + 2 * XB), "s"(slabs + 3 * XB), "s"(slabs + 4 * XB), "s"(slabs + 5 * XB), "s"(slabs + 6 * XB), "s"(slabs + 7 * XB)
                   : "memory", "scc");
#pragma unroll
      for (int k = 0; k < 8; ++k) if (__all(!q3 || v[k][3] == tag)) need &= ~(1u << k);
      need = __builtin_amdgcn_readfirstlane(need);
      if (!need) break;
      __builtin_amdgcn_s_sleep(PSL);
    }
    TSTAMP(9);
    float s[8] = {};
#pragma unroll
    for (int k = 0; k < 8; ++k)
#pragma unroll
      for (int e = 0; e < 4; ++e) { s[2 * e] += __builtin_bit_cast(float, v[k][e] << 16); s[2 * e + 1] += __builtin_bit_cast(float, v[k][e] & 0xffff0000u); }
    const int urp = u < 30 ? u / 10 : 3, ukh = u < 30 ? (u / 5) & 1 : (u - 30) / 3, us = u < 30 ? u % 5 : (u - 30) % 3;
    const bool full = u < 30 ? us < 4 : us < 2;
    u16* ob = out + (size_t)(b * 112 + urp * 32) * 576 + nq * 144 + g * 4;
#pragma unroll
    for (int i = 0; i < 4; ++i) {
      int ep, lr = l15; bool valid;
      if (full) { ep = 4 * us + i; valid = !(q3 && i == 3); }
      else if (u < 30) { ep = i < 2 ? 16 + i : 4 * m4 + 3; if (i == 2) lr = l15 | 3; valid = i < 3; }
      else { ep = i == 0 ? 8 : 4 * m4 + 3; if (i == 1) lr = l15 | 3; valid = i == 0 || (i == 1 && m4 < 2); }
      const int r = ep >= 9 ? 1 : 0, e = 9 * ukh + ep - 9 * r;
      if (valid) *(unsigned*)(ob + (r * 16 + lr) * 576 + (e >> 1) * 16 + (e & 1) * 2) = pk2(s[2 * i], s[2 * i + 1]);
    }
  }
  STAMP(22);
  STAMPFLUSH;
}

extern "C" __global__ void kf_code_end() {}

extern "C" void kernel_launch(void* const* d_in, const int* in_sizes, int n_in, void* d_out, int out_size, void* d_ws, size_t ws_size, hipStream_t stream) {
  const u16* x = (const u16*)d_in[0]; const u16* k = (const u16*)d_in[1]; const u16* v = (const u16*)d_in[2];
  const u16* wq = (const u16*)d_in[3]; const u16* wo = (const u16*)d_in[4];
  static bool init = false;
  if (!init) {
    (void)hipFuncSetAttribute((const void*)k_fused, hipFuncAttributeMaxDynamicSharedMemorySize, 163840);
    init = true;
  }
  k_fused<<<256, 512, 163840, stream>>>(x, k, v, wq, wo, (char*)d_ws, (u16*)d_out, (unsigned long long*)((char*)d_ws + (64u << 20)));
}
```
